```python
import torch

def _softmax(x, axis=-1):
    return torch.softmax(x, dim=axis)
def _gelu(x):
    return torch.nn.functional.gelu(x, approximate="tanh")
def _silu(x):
    return torch.nn.functional.silu(x)
def _sigmoid(x):
    return torch.sigmoid(x)
def _layernorm(x, axis=-1, eps=1e-5):
    m = x.mean(dim=axis, keepdim=True)
    v = x.var(dim=axis, keepdim=True, unbiased=False)
    return (x - m) / torch.sqrt(v + eps)
def _scatter_add(tgt, idx, src, axis=0):
    return tgt.clone().index_add_(axis, idx, src)
def _segment_sum(src, seg, n):
    out = torch.zeros((n,) + src.shape[1:], dtype=src.dtype)
    return out.index_add_(0, seg, src)
def _pad(x, pw):
    flat = tuple(v for pair in reversed(pw) for v in pair)
    return torch.nn.functional.pad(x, flat)

kernel_name = 'recurrent_scan__spectral_gfnet__glu_gated_swiglu__prompt'

def setup_inputs():

    _w = (lambda a: (lambda t: t.to(torch.bfloat16) if t.is_floating_point() else t)(torch.as_tensor(a)))
    def _A_setup():
        B, S, D = 1, 2048, 640
        Df = D // 2 + 1
        return {
            "x": _w((lambda *s: torch.randn(*s, dtype=torch.bfloat16) * 0.2)(B, S, D)),
            "w_r": _w((lambda *s: torch.randn(*s, dtype=torch.bfloat16) * 1.6)(S, Df) * 0.02),
            "w_i": _w((lambda *s: torch.randn(*s, dtype=torch.bfloat16) * 1.6)(S, Df) * 0.02),
        }
    _A_in = _A_setup()

    def _B_setup():
        B, S, D, F = 1, 2048, 640, 1536
        return {
            "x": _w((lambda *s: torch.randn(*s, dtype=torch.bfloat16) * 0.2)(B, S, D)),
            "w1": _w((lambda *s: torch.randn(*s, dtype=torch.bfloat16) * 0.8)(D, F) * (2.0 / D) ** 0.5),
            "w3": _w((lambda *s: torch.randn(*s, dtype=torch.bfloat16) * 0.8)(D, F) * (2.0 / D) ** 0.5),
            "w2": _w((lambda *s: torch.randn(*s, dtype=torch.bfloat16) * 0.8)(F, D) * (2.0 / F) ** 0.5),
        }
    _B_in = _B_setup()

    return {
        "A_x": _A_in["x"],
        "A_w_r": _A_in["w_r"],
        "A_w_i": _A_in["w_i"],
        "B_x": _B_in["x"],
        "B_w1": _B_in["w1"],
        "B_w3": _B_in["w3"],
        "B_w2": _B_in["w2"],
    }

def reference(A_x, A_w_r, A_w_i, B_x, B_w1, B_w3, B_w2):
    def _A_ref(x, w_r, w_i):
        B, S, D = x.shape
        X = (lambda x, axes=(-2,-1): torch.fft.rfft2(x.float(), dim=axes))(x, axes=(-2, -1))
        W = w_r + 1j * w_i
        X = X * W
        y = (lambda x, s, axes=(-2,-1): torch.fft.irfft2(x, s=s, dim=axes).to(torch.bfloat16))(X, (S, D), axes=(-2, -1))
        return y

    def _B_ref(x, w1, w3, w2):
        gate = torch.einsum("bsd,df->bsf", x, w1)
        up = torch.einsum("bsd,df->bsf", x, w3)
        h = _silu(gate) * up
        out = torch.einsum("bsf,fd->bsd", h, w2)
        return out

    x = A_x
    xn = _layernorm(x, axis=-1)
    s = _A_ref(xn, A_w_r, A_w_i)
    g = _silu(_B_ref(xn, B_w1, B_w3, B_w2))
    return x + s * g
```

```cpp
#include <hip/hip_runtime.h>
#include <hip/hip_bf16.h>
#include <stdint.h>

typedef float f2 __attribute__((ext_vector_type(2)));
typedef float f4 __attribute__((ext_vector_type(4)));
typedef short bf16x8 __attribute__((ext_vector_type(8)));
typedef unsigned u4 __attribute__((ext_vector_type(4)));
typedef unsigned u2 __attribute__((ext_vector_type(2)));
typedef __bf16 bf2 __attribute__((ext_vector_type(2)));

#define DEV __device__ __forceinline__

#if defined(__HIP_DEVICE_COMPILE__)
#pragma clang attribute push(__attribute__((target("no-packed-fp32-ops"))), apply_to = function)
#endif

constexpr int S_ = 2048, D_ = 640, F_ = 1536, DF_ = 321;

constexpr size_t OFF_XNP = 0;
constexpr size_t OFF_W13 = OFF_XNP + (size_t)S_ * D_ * 2;
constexpr size_t OFF_W2P = OFF_W13 + (size_t)D_ * F_ * 2 * 2;
constexpr size_t OFF_WT = OFF_W2P + (size_t)F_ * D_ * 2;
constexpr size_t OFF_ZT = OFF_WT + (size_t)DF_ * S_ * 4;
constexpr size_t OFF_VT = OFF_ZT + (size_t)D_ * 1024 * 8;
constexpr size_t OFF_HP = OFF_VT + (size_t)D_ * 1024 * 8;
constexpr size_t OFF_S = OFF_HP + (size_t)S_ * F_ * 2;

DEV float bflo(unsigned u) { return __uint_as_float(u << 16); }
DEV float bfhi(unsigned u) { return __uint_as_float(u & 0xffff0000u); }
DEV unsigned pk_bf16(float a, float b) {
  bf2 v;
  v.x = (__bf16)a;
  v.y = (__bf16)b;
  return __builtin_bit_cast(unsigned, v);
}
DEV float rbf(float a) {
  __bf16 v = (__bf16)a;
  return __uint_as_float(((unsigned)__builtin_bit_cast(unsigned short, v)) << 16);
}
DEV f2 cmul(f2 a, f2 w) { return f2{a.x * w.x - a.y * w.y, a.x * w.y + a.y * w.x}; }
DEV f2 cmulc(f2 a, f2 w) { return f2{a.x * w.x + a.y * w.y, a.y * w.x - a.x * w.y}; }
DEV f2 twid(float x) { return f2{__builtin_amdgcn_cosf(x), -__builtin_amdgcn_sinf(x)}; }
template <int SGN>
DEV f2 muli(f2 a) {
  if (SGN < 0) return f2{a.y, -a.x};
  return f2{-a.y, a.x};
}
template <int SGN>
DEV void dft4(f2& a, f2& b, f2& c, f2& d) {
  f2 t0 = a + c, t1 = a - c, t2 = b + d, t3 = muli<SGN>(b - d);
  a = t0 + t2;
  c = t0 - t2;
  b = t1 + t3;
  d = t1 - t3;
}
template <int SGN>
DEV void dft8(f2* x) {
  f2 e0 = x[0], e1 = x[2], e2 = x[4], e3 = x[6];
  f2 o0 = x[1], o1 = x[3], o2 = x[5], o3 = x[7];
  dft4<SGN>(e0, e1, e2, e3);
  dft4<SGN>(o0, o1, o2, o3);
  const float r = 0.70710678118654752440f;
  f2 m1 = muli<SGN>(o1);
  o1 = (o1 + m1) * r;
  o2 = muli<SGN>(o2);
  f2 m3 = muli<SGN>(o3);
  o3 = (m3 - o3) * r;
  x[0] = e0 + o0; x[4] = e0 - o0;
  x[1] = e1 + o1; x[5] = e1 - o1;
  x[2] = e2 + o2; x[6] = e2 - o2;
  x[3] = e3 + o3; x[7] = e3 - o3;
}
template <int SGN>
DEV void dft5(f2 x0, f2 x1, f2 x2, f2 x3, f2 x4, f2& y0, f2& y1, f2& y2, f2& y3, f2& y4) {
  const float c1 = 0.30901699437494742410f, c2 = -0.80901699437494742410f;
  const float s1 = 0.95105651629515357212f, s2 = 0.58778525229247312917f;
  f2 t1 = x1 + x4, t2 = x2 + x3, t3 = x1 - x4, t4 = x2 - x3;
  y0 = x0 + t1 + t2;
  f2 a1 = x0 + c1 * t1 + c2 * t2, a2 = x0 + c2 * t1 + c1 * t2;
  f2 b1 = muli<SGN>(s1 * t3 + s2 * t4), b2 = muli<SGN>(s2 * t3 - s1 * t4);
  y1 = a1 + b1; y4 = a1 - b1;
  y2 = a2 + b2; y3 = a2 - b2;
}
template <int SGN>
DEV void dft10(f2* x) {
  f2 p[5], m[5];
#pragma unroll
  for (int n2 = 0; n2 < 5; ++n2) {
    f2 a = x[(2 * n2) % 10], b = x[(5 + 2 * n2) % 10];
    p[n2] = a + b;
    m[n2] = a - b;
  }
  dft5<SGN>(p[0], p[1], p[2], p[3], p[4], x[0], x[6], x[2], x[8], x[4]);
  dft5<SGN>(m[0], m[1], m[2], m[3], m[4], x[5], x[1], x[7], x[3], x[9]);
}
DEV void wave_sync() {
  __builtin_amdgcn_fence(__ATOMIC_RELEASE, "wavefront");
  __builtin_amdgcn_wave_barrier();
  __builtin_amdgcn_fence(__ATOMIC_ACQUIRE, "wavefront");
}
DEV float silu_f(float g) { return g * __builtin_amdgcn_rcpf(1.0f + __expf(-g)); }

constexpr int K0_THREADS = 320;
constexpr int K0_FFT_BLOCKS = 256;
constexpr int K0_W13_BLOCKS = (D_ * F_ * 2 / 64) / K0_THREADS;
constexpr int K0_W2_BLOCKS = (F_ * D_ / 64) / K0_THREADS;
constexpr int K0_WT_BLOCKS = (DF_ * 64 + K0_THREADS - 1) / K0_THREADS;
constexpr int ROW_P1 = 106, ROW_RS1 = 848;
constexpr int ROW_P2 = 12, ROW_RS2 = 769;

DEV void tr8x8(const u4* r, u4* o) {
#pragma unroll
  for (int c2 = 0; c2 < 4; ++c2) {
#pragma unroll
    for (int k2 = 0; k2 < 4; ++k2) {
      unsigned a = r[2 * k2][c2], b = r[2 * k2 + 1][c2];
      o[2 * c2][k2] = __builtin_amdgcn_perm(b, a, 0x05040100u);
      o[2 * c2 + 1][k2] = __builtin_amdgcn_perm(b, a, 0x07060302u);
    }
  }
}

__global__ __launch_bounds__(K0_THREADS) void k0_kernel(const unsigned short* __restrict__ x,
                                                        const unsigned short* __restrict__ wr,
                                                        const unsigned short* __restrict__ wi,
                                                        const unsigned short* __restrict__ w1,
                                                        const unsigned short* __restrict__ w3,
                                                        const unsigned short* __restrict__ w2, char* __restrict__ ws) {
  __shared__ __attribute__((aligned(16))) float ldsA[4 * ROW_RS2 * 2 + 64];
  __shared__ __attribute__((aligned(16))) f2 ldsB[4 * ROW_RS1];
  const int tid = threadIdx.x;
  int b = blockIdx.x;
  if (b < K0_FFT_BLOCKS) {
    const int g = (b & 7) * 32 + (b >> 3);
    const int wv = tid >> 6, lane = tid & 63;
    if (wv < 4) {
      const int half = lane >> 5, c = lane & 31;
      const int tok = 4 * g + wv + 1024 * half;
      const u2* xr = (const u2*)(x + (size_t)tok * D_);
      u2 v[5];
#pragma unroll
      for (int i = 0; i < 5; ++i) v[i] = xr[c + 32 * i];
      float f[20];
#pragma unroll
      for (int i = 0; i < 5; ++i) {
        f[4 * i + 0] = bflo(v[i].x); f[4 * i + 1] = bfhi(v[i].x);
        f[4 * i + 2] = bflo(v[i].y); f[4 * i + 3] = bfhi(v[i].y);
      }
      float sum = 0.f;
#pragma unroll
      for (int i = 0; i < 20; ++i) sum += f[i];
#pragma unroll
      for (int o = 1; o < 32; o <<= 1) sum += __shfl_xor(sum, o);
      const float mean = sum * (1.0f / D_);
      float sq = 0.f;
#pragma unroll
      for (int i = 0; i < 20; ++i) { float d = f[i] - mean; sq += d * d; }
#pragma unroll
      for (int o = 1; o < 32; o <<= 1) sq += __shfl_xor(sq, o);
      const float mb = rbf(mean);
      const float vb = rbf(sq * (1.0f / D_));
      const float den = rbf(sqrtf(rbf(vb + 1e-5f)));
      const float rc = __builtin_amdgcn_rcpf(den);
      float* pl = ldsA + (wv * 2 + half) * D_;
      char* xnp = ws + OFF_XNP;
#pragma unroll
      for (int i = 0; i < 5; ++i) {
        float q[4];
#pragma unroll
        for (int e = 0; e < 4; ++e) {
          float d = rbf(f[4 * i + e] - mb);
          float t = d * rc;
          float er = __builtin_fmaf(-t, den, d);
          t = __builtin_fmaf(er, rc, t);
          q[e] = t;
        }
        u2 o;
        o.x = pk_bf16(q[0], q[1]);
        o.y = pk_bf16(q[2], q[3]);
        const int n = 4 * (c + 32 * i);
        const int ks = n >> 5, qq = (n >> 3) & 3, hf = (n >> 2) & 1;
        size_t off = ((((size_t)(tok >> 7) * 20 + ks) * 8 + ((tok >> 4) & 7)) * 64 + qq * 16 + (tok & 15)) * 16 + hf * 8;
        *(u2*)(xnp + off) = o;
        f4 w;
        w.x = bflo(o.x); w.y = bfhi(o.x); w.z = bflo(o.y); w.w = bfhi(o.y);
        *(f4*)(pl + n) = w;
      }
    }
    __syncthreads();
    const int r = tid / 80, i80 = tid - r * 80;
    {
      const int m = i80;
      f2 z[8];
      const float* pr = ldsA + (r * 2) * D_;
      const float* pi = pr + D_;
#pragma unroll
      for (int n1 = 0; n1 < 8; ++n1) z[n1] = f2{pr[80 * n1 + m], pi[80 * n1 + m]};
      dft8<-1>(z);
      f2* o = ldsB + r * ROW_RS1 + m;
      o[0] = z[0];
#pragma unroll
      for (int k1 = 1; k1 < 8; ++k1) o[k1 * ROW_P1] = cmul(z[k1], twid((float)(m * k1) * (1.0f / 640.0f)));
    }
    __syncthreads();
    {
      const int k1 = i80 / 10, n3 = i80 - k1 * 10;
      f2 z[8];
      const f2* p = ldsB + r * ROW_RS1 + k1 * ROW_P1 + n3;
#pragma unroll
      for (int n2 = 0; n2 < 8; ++n2) z[n2] = p[10 * n2];
      dft8<-1>(z);
      f2* o = (f2*)ldsA + r * ROW_RS2 + k1 * ROW_P2 + n3;
      o[0] = z[0];
#pragma unroll
      for (int k2 = 1; k2 < 8; ++k2) o[8 * k2 * ROW_P2] = cmul(z[k2], twid((float)(n3 * k2) * (1.0f / 80.0f)));
    }
    __syncthreads();
    if (tid < 256) {
      const int rr = tid & 3, j = tid >> 2;
      f2 z[10];
      const f2* p = (const f2*)ldsA + rr * ROW_RS2 + j * ROW_P2;
#pragma unroll
      for (int n3 = 0; n3 < 10; ++n3) z[n3] = p[n3];
      dft10<-1>(z);
      f2* zt = (f2*)(ws + OFF_ZT);
#pragma unroll
      for (int k3 = 0; k3 < 10; ++k3) zt[(size_t)(j + 64 * k3) * 1024 + 4 * g + rr] = z[k3];
    }
    return;
  }
  b -= K0_FFT_BLOCKS;
  if (b < K0_W13_BLOCKS) {
    const int t = b * K0_THREADS + tid;
    const int fg = t % 192, kg = (t / 192) % 80, mat = t / (192 * 80);
    const unsigned short* w = mat ? w3 : w1;
    u4 rr[8], o[8];
#pragma unroll
    for (int k = 0; k < 8; ++k) rr[k] = *(const u4*)(w + (size_t)(kg * 8 + k) * F_ + fg * 8);
    tr8x8(rr, o);
    const int f0 = fg * 8, nb = f0 / 96, ft = (f0 % 96) >> 4, row0 = f0 & 15;
    const int ks = kg >> 2, q = kg & 3;
    char* dst = ws + OFF_W13 + ((((size_t)nb * 20 + ks) * 12 + ft * 2 + mat) * 64 + q * 16 + row0) * 16;
#pragma unroll
    for (int c = 0; c < 8; ++c) *(u4*)(dst + c * 16) = o[c];
    return;
  }
  b -= K0_W13_BLOCKS;
  if (b < K0_W2_BLOCKS) {
    const int t = b * K0_THREADS + tid;
    const int dg = t % 80, kq = t / 80;
    const int ks = kq >> 2, q = kq & 3;
    u4 rr[8], o[8];
#pragma unroll
    for (int j = 0; j < 8; ++j) {
      const int f = 32 * ks + 16 * (j >> 2) + 4 * q + (j & 3);
      rr[j] = *(const u4*)(w2 + (size_t)f * D_ + dg * 8);
    }
    tr8x8(rr, o);
    const int d0 = dg * 8, nb = d0 / 80, dt = (d0 % 80) >> 4, row0 = d0 & 15;
    char* dst = ws + OFF_W2P + ((((size_t)nb * 48 + ks) * 5 + dt) * 64 + q * 16 + row0) * 16;
#pragma unroll
    for (int c = 0; c < 8; ++c) *(u4*)(dst + c * 16) = o[c];
    return;
  }
  b -= K0_W2_BLOCKS;
  {
    const int t = b * K0_THREADS + tid;
    if (t >= DF_ * 64) return;
    const int col = t % DF_, grp = t / DF_;
    unsigned o[32];
#pragma unroll
    for (int a = 0; a < 4; ++a) {
      const int tt = grp * 4 + a;
      const int k1 = tt >> 6, k2 = (tt >> 3) & 7, k3 = tt & 7;
#pragma unroll
      for (int k4 = 0; k4 < 8; ++k4) {
        const int k = k1 + 4 * k2 + 32 * k3 + 256 * k4;
        unsigned re = wr[(size_t)k * DF_ + col], im = wi[(size_t)k * DF_ + col];
        o[a * 8 + k4] = re | (im << 16);
      }
    }
    u4* dst = (u4*)(ws + OFF_WT + ((size_t)col * S_ + grp * 32) * 4);
#pragma unroll
    for (int i = 0; i < 8; ++i) dst[i] = u4{o[4 * i], o[4 * i + 1], o[4 * i + 2], o[4 * i + 3]};
  }
}

constexpr int G1_NS = 4;
constexpr int G1_STAGE = 20 * 1024;
constexpr int K1_LDS = G1_NS * G1_STAGE;
constexpr int COL_WS = 640;

#define STNOP
DEV void colfft_block(int col, char* __restrict__ ws, f2* lds) {
  const int tid = threadIdx.x, wv = tid >> 6, lane = tid & 63;
  const int cb = (col == 0 || col == 320) ? col : 640 - col;
  const f4* za = (const f4*)(ws + OFF_ZT) + (size_t)col * 512;
  const f4* zb = (const f4*)(ws + OFF_ZT) + (size_t)cb * 512;
  f4 a0 = za[tid], a1 = za[tid + 256], b0 = zb[tid], b1 = zb[tid + 256];
  const u4* wt = (const u4*)(ws + OFF_WT + ((size_t)col * S_ + tid * 8) * 4);
  u4 wq0 = wt[0], wq1 = wt[1];
  f2 t1[2][3], t2[7], t3[7];
#pragma unroll
  for (int e = 0; e < 2; ++e)
#pragma unroll
    for (int k1 = 1; k1 < 4; ++k1) t1[e][k1 - 1] = twid((float)((2 * tid + e) * k1) * (1.0f / 2048.0f));
#pragma unroll
  for (int k = 1; k < 8; ++k) t2[k - 1] = twid((float)(lane * k) * (1.0f / 512.0f));
#pragma unroll
  for (int k = 1; k < 8; ++k) t3[k - 1] = twid((float)((lane & 7) * k) * (1.0f / 64.0f));
  f2 xx[2][4];
  {
    f2 za00 = f2{a0.x, a0.y}, za01 = f2{a0.z, a0.w}, za10 = f2{a1.x, a1.y}, za11 = f2{a1.z, a1.w};
    f2 zb00 = f2{b0.x, b0.y}, zb01 = f2{b0.z, b0.w}, zb10 = f2{b1.x, b1.y}, zb11 = f2{b1.z, b1.w};
#define UNPK(ZA, ZB, A, B)                  \
  A = f2{ZA.x + ZB.x, ZA.y - ZB.y};         \
  B = f2{ZA.y + ZB.y, ZB.x - ZA.x};
    UNPK(za00, zb00, xx[0][0], xx[0][2])
    UNPK(za01, zb01, xx[1][0], xx[1][2])
    UNPK(za10, zb10, xx[0][1], xx[0][3])
    UNPK(za11, zb11, xx[1][1], xx[1][3])
#undef UNPK
  }
#pragma unroll
  for (int e = 0; e < 2; ++e) {
    dft4<-1>(xx[e][0], xx[e][1], xx[e][2], xx[e][3]);
#pragma unroll
    for (int k1 = 1; k1 < 4; ++k1) xx[e][k1] = cmul(xx[e][k1], t1[e][k1 - 1]);
  }
#pragma unroll
  for (int k1 = 0; k1 < 4; ++k1)
  { *(f4*)(lds + k1 * COL_WS + 2 * tid) = f4{xx[0][k1].x, xx[0][k1].y, xx[1][k1].x, xx[1][k1].y}; STNOP }
  __syncthreads();
  f2* L = lds + wv * COL_WS;
  f2 z[8];
#pragma unroll
  for (int n2 = 0; n2 < 8; ++n2) z[n2] = L[64 * n2 + lane];
  dft8<-1>(z);
#pragma unroll
  for (int k = 1; k < 8; ++k) z[k] = cmul(z[k], t2[k - 1]);
  wave_sync();
#pragma unroll
  for (int k2 = 0; k2 < 8; ++k2) { L[72 * k2 + lane] = z[k2]; STNOP }
  wave_sync();
  const int hi = lane >> 3, lo = lane & 7;
#pragma unroll
  for (int n3 = 0; n3 < 8; ++n3) z[n3] = L[72 * hi + 8 * n3 + lo];
  dft8<-1>(z);
#pragma unroll
  for (int k = 1; k < 8; ++k) z[k] = cmul(z[k], t3[k - 1]);
  wave_sync();
#pragma unroll
  for (int k3 = 0; k3 < 8; ++k3) { L[80 * hi + 10 * k3 + lo] = z[k3]; STNOP }
  wave_sync();
#pragma unroll
  for (int c = 0; c < 4; ++c) {
    f4 v = *(const f4*)(L + 10 * lane + 2 * c);
    z[2 * c] = f2{v.x, v.y};
    z[2 * c + 1] = f2{v.z, v.w};
  }
  dft8<-1>(z);
  {
    const float sc = 0.5f / (2048.0f * 640.0f);
    unsigned wv8[8] = {wq0.x, wq0.y, wq0.z, wq0.w, wq1.x, wq1.y, wq1.z, wq1.w};
#pragma unroll
    for (int k4 = 0; k4 < 8; ++k4) z[k4] = cmul(z[k4], f2{bflo(wv8[k4]) * sc, bfhi(wv8[k4]) * sc});
  }
  dft8<1>(z);
  wave_sync();
#pragma unroll
  for (int c = 0; c < 4; ++c)
  { *(f4*)(L + 10 * lane + 2 * c) = f4{z[2 * c].x, z[2 * c].y, z[2 * c + 1].x, z[2 * c + 1].y}; STNOP }
  wave_sync();
#pragma unroll
  for (int k3 = 0; k3 < 8; ++k3) z[k3] = L[80 * hi + 10 * k3 + lo];
#pragma unroll
  for (int k = 1; k < 8; ++k) z[k] = cmulc(z[k], t3[k - 1]);
  dft8<1>(z);
  wave_sync();
#pragma unroll
  for (int n3 = 0; n3 < 8; ++n3) { L[72 * hi + 8 * n3 + lo] = z[n3]; STNOP }
  wave_sync();
#pragma unroll
  for (int k2 = 0; k2 < 8; ++k2) z[k2] = L[72 * k2 + lane];
#pragma unroll
  for (int k = 1; k < 8; ++k) z[k] = cmulc(z[k], t2[k - 1]);
  dft8<1>(z);
  wave_sync();
#pragma unroll
  for (int n2 = 0; n2 < 8; ++n2) { L[64 * n2 + lane] = z[n2]; STNOP }
  __syncthreads();
#pragma unroll
  for (int k1 = 0; k1 < 4; ++k1) {
    f4 v = *(const f4*)(lds + k1 * COL_WS + 2 * tid);
    xx[0][k1] = f2{v.x, v.y};
    xx[1][k1] = f2{v.z, v.w};
  }
#pragma unroll
  for (int e = 0; e < 2; ++e) {
#pragma unroll
    for (int k1 = 1; k1 < 4; ++k1) xx[e][k1] = cmulc(xx[e][k1], t1[e][k1 - 1]);
    dft4<1>(xx[e][0], xx[e][1], xx[e][2], xx[e][3]);
  }
  f4* va = (f4*)(ws + OFF_VT) + (size_t)col * 512;
  f4* vb = (f4*)(ws + OFF_VT) + (size_t)cb * 512;
  if (col == 0 || col == 320) {
#pragma unroll
    for (int j = 0; j < 2; ++j)
      va[tid + 256 * j] = f4{xx[0][j].x, xx[0][j + 2].x, xx[1][j].x, xx[1][j + 2].x};
  } else {
#pragma unroll
    for (int j = 0; j < 2; ++j) {
      f2 A0 = xx[0][j], B0 = xx[0][j + 2], A1 = xx[1][j], B1 = xx[1][j + 2];
      va[tid + 256 * j] = f4{A0.x - B0.y, A0.y + B0.x, A1.x - B1.y, A1.y + B1.x};
      vb[tid + 256 * j] = f4{A0.x + B0.y, B0.x - A0.y, A1.x + B1.y, B1.x - A1.y};
    }
  }
}

#define WAITV(n) asm volatile("s_waitcnt vmcnt(" #n ")" ::: "memory")

__global__ __launch_bounds__(256, 2) void k1_kernel(char* __restrict__ ws, int boff) {
  extern __shared__ __attribute__((aligned(16))) char smem[];
  const int b = blockIdx.x + boff;
  if (b >= 256) {
    colfft_block(b - 256, ws, (f2*)smem);
    return;
  }
  const int tid = threadIdx.x, wv = tid >> 6, lane = tid & 63;
  const int xc = b & 7, idx = b >> 3;
  const int mb = (xc & 1) * 8 + (idx & 7), nb = (xc >> 1) * 4 + (idx >> 3);
  const char* gw = ws + OFF_W13 + (size_t)nb * 20 * 12 * 1024;
  const char* gx = ws + OFF_XNP + (size_t)mb * 20 * 8 * 1024;
  auto stage = [&](int ks) {
    char* dst = smem + (ks % G1_NS) * G1_STAGE;
#pragma unroll
    for (int i = 0; i < 5; ++i) {
      const int fr = wv * 5 + i;
      const char* src = (fr < 12) ? gw + ((size_t)ks * 12 + fr) * 1024 : gx + ((size_t)ks * 8 + (fr - 12)) * 1024;
      __builtin_amdgcn_global_load_lds((const unsigned*)(src + lane * 16), (unsigned*)(dst + fr * 1024), 16, 0, 0);
    }
  };
  const int wm = wv >> 1, wn = wv & 1;
  f4 acc[3][2][4];
#pragma unroll
  for (int i = 0; i < 3; ++i)
#pragma unroll
    for (int j = 0; j < 2; ++j)
#pragma unroll
      for (int t = 0; t < 4; ++t) acc[i][j][t] = f4{0.f, 0.f, 0.f, 0.f};
  stage(0);
  stage(1);
  stage(2);
#pragma unroll
  for (int ks = 0; ks < 20; ++ks) {
    if (ks <= 17) { WAITV(10); } else if (ks == 18) { WAITV(5); } else { WAITV(0); }
    asm volatile("s_waitcnt lgkmcnt(0)" ::: "memory");
    __builtin_amdgcn_s_barrier();
    asm volatile("" ::: "memory");
    if (ks + 3 < 20) stage(ks + 3);
    const char* sb = smem + (ks % G1_NS) * G1_STAGE;
    bf16x8 a[3][2], bt[4];
#pragma unroll
    for (int i = 0; i < 3; ++i)
#pragma unroll
      for (int j = 0; j < 2; ++j) a[i][j] = *(const bf16x8*)(sb + ((wn * 3 + i) * 2 + j) * 1024 + lane * 16);
#pragma unroll
    for (int t = 0; t < 4; ++t) bt[t] = *(const bf16x8*)(sb + (12 + wm * 4 + t) * 1024 + lane * 16);
#pragma unroll
    for (int i = 0; i < 3; ++i)
#pragma unroll
      for (int j = 0; j < 2; ++j)
#pragma unroll
        for (int t = 0; t < 4; ++t)
          acc[i][j][t] = __builtin_amdgcn_mfma_f32_16x16x32_bf16(a[i][j], bt[t], acc[i][j][t], 0, 0, 0);
  }
#pragma unroll
  for (int t = 0; t < 4; ++t) {
    unsigned hw[3][2];
#pragma unroll
    for (int i = 0; i < 3; ++i) {
      float hv[4];
#pragma unroll
      for (int r = 0; r < 4; ++r) {
        float gg = rbf(acc[i][0][t][r]), uu = rbf(acc[i][1][t][r]);
        hv[r] = rbf(silu_f(gg)) * uu;
      }
      hw[i][0] = pk_bf16(hv[0], hv[1]);
      hw[i][1] = pk_bf16(hv[2], hv[3]);
    }
    char* base = ws + OFF_HP + ((((size_t)(mb * 2 + wm) * 48 + nb * 3) * 4 + t) * 64 + lane) * 16;
    if (wn == 0) {
      *(u4*)(base) = u4{hw[0][0], hw[0][1], hw[1][0], hw[1][1]};
      *(u2*)(base + 4096) = u2{hw[2][0], hw[2][1]};
    } else {
      *(u2*)(base + 4096 + 8) = u2{hw[0][0], hw[0][1]};
      *(u4*)(base + 8192) = u4{hw[1][0], hw[1][1], hw[2][0], hw[2][1]};
    }
  }
}

__global__ __launch_bounds__(K0_THREADS) void k2_kernel(char* __restrict__ ws) {
  __shared__ __attribute__((aligned(16))) f2 ldsA[4 * ROW_RS2 + 32];
  __shared__ __attribute__((aligned(16))) f2 ldsB[4 * ROW_RS1];
  const int tid = threadIdx.x, b = blockIdx.x;
  const int g = (b & 7) * 32 + (b >> 3);
  if (tid < 256) {
    const int rr = tid & 3, j = tid >> 2;
    const f2* vt = (const f2*)(ws + OFF_VT);
    f2 z[10];
#pragma unroll
    for (int k3 = 0; k3 < 10; ++k3) z[k3] = vt[(size_t)(j + 64 * k3) * 1024 + 4 * g + rr];
    dft10<1>(z);
    f2* p = ldsA + rr * ROW_RS2 + j * ROW_P2;
#pragma unroll
    for (int n3 = 0; n3 < 10; ++n3) p[n3] = z[n3];
  }
  __syncthreads();
  const int r = tid / 80, i80 = tid - r * 80;
  {
    const int k1 = i80 / 10, n3 = i80 - k1 * 10;
    f2 z[8];
    const f2* p = ldsA + r * ROW_RS2 + k1 * ROW_P2 + n3;
    z[0] = p[0];
#pragma unroll
    for (int k2 = 1; k2 < 8; ++k2) z[k2] = cmulc(p[8 * k2 * ROW_P2], twid((float)(n3 * k2) * (1.0f / 80.0f)));
    dft8<1>(z);
    f2* o = ldsB + r * ROW_RS1 + k1 * ROW_P1 + n3;
#pragma unroll
    for (int n2 = 0; n2 < 8; ++n2) o[10 * n2] = z[n2];
  }
  __syncthreads();
  {
    const int m = i80;
    f2 z[8];
    const f2* p = ldsB + r * ROW_RS1 + m;
    z[0] = p[0];
#pragma unroll
    for (int k1 = 1; k1 < 8; ++k1) z[k1] = cmulc(p[k1 * ROW_P1], twid((float)(m * k1) * (1.0f / 640.0f)));
    dft8<1>(z);
    unsigned short* sa = (unsigned short*)(ws + OFF_S) + (size_t)(4 * g + r) * D_ + m;
    unsigned short* sb = sa + (size_t)1024 * D_;
#pragma unroll
    for (int n1 = 0; n1 < 8; ++n1) {
      unsigned pk = pk_bf16(z[n1].x, z[n1].y);
      sa[80 * n1] = (unsigned short)(pk & 0xffff);
      sb[80 * n1] = (unsigned short)(pk >> 16);
    }
  }
}

constexpr int G2_DEPTH = 6;
__global__ __launch_bounds__(256, 1) void k3_kernel(const char* __restrict__ ws, const unsigned short* __restrict__ x,
                                                    unsigned short* __restrict__ out) {
  __shared__ __attribute__((aligned(16))) f4 red[4 * 20 * 64];
  const int tid = threadIdx.x, wv = tid >> 6, lane = tid & 63, b = blockIdx.x;
  const int xc = b & 7, idx = b >> 3;
  const int mb = xc * 4 + (idx & 3), nb = idx >> 2;
  const bf16x8* gh = (const bf16x8*)(ws + OFF_HP) + ((size_t)mb * 48 + wv * 12) * 4 * 64 + lane;
  const bf16x8* gw = (const bf16x8*)(ws + OFF_W2P) + ((size_t)nb * 48 + wv * 12) * 5 * 64 + lane;
  f4 acc[5][4];
#pragma unroll
  for (int i = 0; i < 5; ++i)
#pragma unroll
    for (int t = 0; t < 4; ++t) acc[i][t] = f4{0.f, 0.f, 0.f, 0.f};
  bf16x8 fa[G2_DEPTH][5], fb[G2_DEPTH][4];
#pragma unroll
  for (int s = 0; s < G2_DEPTH; ++s) {
#pragma unroll
    for (int t = 0; t < 4; ++t) fb[s][t] = gh[(s * 4 + t) * 64];
#pragma unroll
    for (int i = 0; i < 5; ++i) fa[s][i] = gw[(s * 5 + i) * 64];
  }
#pragma unroll
  for (int s = 0; s < 12; ++s) {
    const int sl = s % G2_DEPTH;
#pragma unroll
    for (int i = 0; i < 5; ++i)
#pragma unroll
      for (int t = 0; t < 4; ++t)
        acc[i][t] = __builtin_amdgcn_mfma_f32_16x16x32_bf16(fa[sl][i], fb[sl][t], acc[i][t], 0, 0, 0);
    if (s + G2_DEPTH < 12) {
#pragma unroll
      for (int t = 0; t < 4; ++t) fb[sl][t] = gh[((s + G2_DEPTH) * 4 + t) * 64];
#pragma unroll
      for (int i = 0; i < 5; ++i) fa[sl][i] = gw[((s + G2_DEPTH) * 5 + i) * 64];
    }
  }
#pragma unroll
  for (int t = 0; t < 4; ++t)
#pragma unroll
    for (int i = 0; i < 5; ++i) red[((wv * 4 + t) * 5 + i) * 64 + lane] = acc[i][t];
  __syncthreads();
  const int tok = mb * 64 + wv * 16 + (lane & 15);
  const int q = lane >> 4;
#pragma unroll
  for (int i = 0; i < 5; ++i) {
    f4 v = red[((0 * 4 + wv) * 5 + i) * 64 + lane];
#pragma unroll
    for (int w = 1; w < 4; ++w) v += red[((w * 4 + wv) * 5 + i) * 64 + lane];
    const size_t o = (size_t)tok * D_ + nb * 80 + i * 16 + q * 4;
    u2 xv = *(const u2*)(x + o);
    u2 sv = *(const u2*)((const unsigned short*)(ws + OFF_S) + o);
    float xs[4] = {bflo(xv.x), bfhi(xv.x), bflo(xv.y), bfhi(xv.y)};
    float ss[4] = {bflo(sv.x), bfhi(sv.x), bflo(sv.y), bfhi(sv.y)};
    float res[4];
#pragma unroll
    for (int r = 0; r < 4; ++r) {
      float gg = rbf(silu_f(rbf(v[r])));
      res[r] = xs[r] + rbf(ss[r] * gg);
    }
    *(u2*)(out + o) = u2{pk_bf16(res[0], res[1]), pk_bf16(res[2], res[3])};
  }
}

#if defined(__HIP_DEVICE_COMPILE__)
#pragma clang attribute pop
#endif

extern "C" void kernel_launch(void* const* d_in, const int* in_sizes, int n_in, void* d_out, int out_size, void* d_ws,
                              size_t ws_size, hipStream_t stream) {
  const unsigned short* x = (const unsigned short*)d_in[0];
  const unsigned short* wr = (const unsigned short*)d_in[1];
  const unsigned short* wi = (const unsigned short*)d_in[2];
  const unsigned short* w1 = (const unsigned short*)d_in[4];
  const unsigned short* w3 = (const unsigned short*)d_in[5];
  const unsigned short* w2 = (const unsigned short*)d_in[6];
  char* ws = (char*)d_ws;
  (void)hipFuncSetAttribute((const void*)k1_kernel, hipFuncAttributeMaxDynamicSharedMemorySize, K1_LDS);
  k0_kernel<<<K0_FFT_BLOCKS + K0_W13_BLOCKS + K0_W2_BLOCKS + K0_WT_BLOCKS, K0_THREADS, 0, stream>>>(x, wr, wi, w1, w3,
                                                                                                    w2, ws);
  k1_kernel<<<256 + DF_, 256, K1_LDS, stream>>>(ws, 0);
  k2_kernel<<<256, K0_THREADS, 0, stream>>>(ws);
  k3_kernel<<<256, 256, 0, stream>>>(ws, x, (unsigned short*)d_out);
}
```

```cpp
#include <hip/hip_runtime.h>
#include <hip/hip_bf16.h>
#include <stdint.h>

typedef float f2 __attribute__((ext_vector_type(2)));
typedef float f4 __attribute__((ext_vector_type(4)));
typedef short bf16x8 __attribute__((ext_vector_type(8)));
typedef short s4v __attribute__((ext_vector_type(4)));
typedef unsigned u4 __attribute__((ext_vector_type(4)));
typedef unsigned u2 __attribute__((ext_vector_type(2)));
typedef __bf16 bf2 __attribute__((ext_vector_type(2)));

#define DEV __device__ __forceinline__

#if defined(__HIP_DEVICE_COMPILE__)
#pragma clang attribute push(__attribute__((target("no-packed-fp32-ops"))), apply_to = function)
#endif

constexpr int S_ = 2048, D_ = 640, F_ = 1536, DF_ = 321;

constexpr size_t OFF_CNT = 0;
constexpr size_t OFF_WMAX = 16384;
constexpr size_t OFF_XNP = 32768;
constexpr size_t OFF_W2P = OFF_XNP + (size_t)S_ * D_ * 2;
constexpr size_t OFF_WT = OFF_W2P + (size_t)F_ * D_ * 2;
constexpr size_t OFF_XT = OFF_WT + (size_t)DF_ * S_ * 4;
constexpr size_t OFF_VT = OFF_XT + (size_t)DF_ * S_ * 4;
constexpr size_t OFF_HP = OFF_VT + (size_t)D_ * 1024 * 4;
constexpr size_t OFF_S = OFF_HP + (size_t)S_ * F_ * 2;
constexpr size_t WS_USED = OFF_S + (size_t)S_ * D_ * 2;

#define TIDX ((int)__builtin_amdgcn_workitem_id_x())
#define BIDX ((int)__builtin_amdgcn_workgroup_id_x())
#define U2F(x) __builtin_bit_cast(float, (unsigned)(x))
#define F2U(x) __builtin_bit_cast(unsigned, (float)(x))
#define CFENCE asm volatile("" ::: "memory")
#define WAITV(n)                                                                        \
  do {                                                                                  \
    CFENCE;                                                                             \
    __builtin_amdgcn_s_waitcnt(((n) & 15) | (((n) >> 4) << 14) | (7 << 4) | (15 << 8)); \
    CFENCE;                                                                             \
  } while (0)
#define WAITL0                          \
  do {                                  \
    CFENCE;                             \
    __builtin_amdgcn_s_waitcnt(0xC07F); \
    CFENCE;                             \
  } while (0)
#define BARRIER                   \
  do {                            \
    CFENCE;                       \
    __builtin_amdgcn_s_barrier(); \
    CFENCE;                       \
  } while (0)
#define GLDS(g, l, off) __builtin_amdgcn_global_load_lds((const unsigned*)(g), (unsigned*)(l), 16, off, 0)
#define GLDSB(voff, sbase, ldsreg, imm)                                                                   \
  asm volatile("s_add_i32 m0, %2, %3\n\ts_nop 0\n\tglobal_load_lds_dwordx4 %0, %1" ::"v"(voff), "s"(sbase), \
               "s"(ldsreg), "i"(imm)                                                                      \
               : "memory", "scc")
#define WAITVA(n) asm volatile("s_waitcnt vmcnt(" #n ")" ::: "memory")
#define WAITVA_I(n) asm volatile("s_waitcnt vmcnt(%0)" ::"i"(n) : "memory")
#define SCHED0 __builtin_amdgcn_sched_barrier(0)
DEV void block_sync() {
  WAITL0;
  BARRIER;
}
template <int CTRL>
DEV float dpp_f(float v) {
  return U2F(__builtin_amdgcn_update_dpp(0, (int)F2U(v), CTRL, 0xf, 0xf, false));
}
DEV float sum32(float v) {
  v += dpp_f<0x121>(v);
  v += dpp_f<0x122>(v);
  v += dpp_f<0x124>(v);
  v += dpp_f<0x128>(v);
  const auto sw = __builtin_amdgcn_permlane16_swap(F2U(v), F2U(v), false, false);
  return U2F(sw[0]) + U2F(sw[1]);
}
DEV int max64(int v) {
#define MX_STEP(C) { const int t = __builtin_amdgcn_update_dpp(0, v, C, 0xf, 0xf, false); v = t > v ? t : v; }
  MX_STEP(0x121) MX_STEP(0x122) MX_STEP(0x124) MX_STEP(0x128)
#undef MX_STEP
  const auto a = __builtin_amdgcn_permlane16_swap((unsigned)v, (unsigned)v, false, false);
  v = (int)a[0] > (int)a[1] ? (int)a[0] : (int)a[1];
  const auto b = __builtin_amdgcn_permlane32_swap((unsigned)v, (unsigned)v, false, false);
  return (int)b[0] > (int)b[1] ? (int)b[0] : (int)b[1];
}
DEV float bflo(unsigned u) { return U2F(u << 16); }
DEV float bfhi(unsigned u) { return U2F(u & 0xffff0000u); }
DEV unsigned pk_bf16(float a, float b) {
  bf2 v;
  v.x = (__bf16)a;
  v.y = (__bf16)b;
  return __builtin_bit_cast(unsigned, v);
}
DEV float rbf(float a) {
  __bf16 v = (__bf16)a;
  return U2F(((unsigned)__builtin_bit_cast(unsigned short, v)) << 16);
}
DEV f2 cmul(f2 a, f2 w) { return f2{a.x * w.x - a.y * w.y, a.x * w.y + a.y * w.x}; }
DEV f2 cmulc(f2 a, f2 w) { return f2{a.x * w.x + a.y * w.y, a.y * w.x - a.x * w.y}; }
DEV f2 twid(float x) { return f2{__builtin_amdgcn_cosf(x), -__builtin_amdgcn_sinf(x)}; }
template <int SGN>
DEV f2 muli(f2 a) {
  if (SGN < 0) return f2{a.y, -a.x};
  return f2{-a.y, a.x};
}
template <int SGN>
DEV void dft4(f2& a, f2& b, f2& c, f2& d) {
  f2 t0 = a + c, t1 = a - c, t2 = b + d, t3 = muli<SGN>(b - d);
  a = t0 + t2;
  c = t0 - t2;
  b = t1 + t3;
  d = t1 - t3;
}
template <int SGN>
DEV void dft8(f2* x) {
  f2 e0 = x[0], e1 = x[2], e2 = x[4], e3 = x[6];
  f2 o0 = x[1], o1 = x[3], o2 = x[5], o3 = x[7];
  dft4<SGN>(e0, e1, e2, e3);
  dft4<SGN>(o0, o1, o2, o3);
  const float r = 0.70710678118654752440f;
  f2 m1 = muli<SGN>(o1);
  o1 = (o1 + m1) * r;
  o2 = muli<SGN>(o2);
  f2 m3 = muli<SGN>(o3);
  o3 = (m3 - o3) * r;
  x[0] = e0 + o0; x[4] = e0 - o0;
  x[1] = e1 + o1; x[5] = e1 - o1;
  x[2] = e2 + o2; x[6] = e2 - o2;
  x[3] = e3 + o3; x[7] = e3 - o3;
}
template <int SGN>
DEV void dft5(f2 x0, f2 x1, f2 x2, f2 x3, f2 x4, f2& y0, f2& y1, f2& y2, f2& y3, f2& y4) {
  const float c1 = 0.30901699437494742410f, c2 = -0.80901699437494742410f;
  const float s1 = 0.95105651629515357212f, s2 = 0.58778525229247312917f;
  f2 t1 = x1 + x4, t2 = x2 + x3, t3 = x1 - x4, t4 = x2 - x3;
  y0 = x0 + t1 + t2;
  f2 a1 = x0 + c1 * t1 + c2 * t2, a2 = x0 + c2 * t1 + c1 * t2;
  f2 b1 = muli<SGN>(s1 * t3 + s2 * t4), b2 = muli<SGN>(s2 * t3 - s1 * t4);
  y1 = a1 + b1; y4 = a1 - b1;
  y2 = a2 + b2; y3 = a2 - b2;
}
DEV void wave_sync() {
  __builtin_amdgcn_fence(__ATOMIC_RELEASE, "wavefront");
  __builtin_amdgcn_wave_barrier();
  __builtin_amdgcn_fence(__ATOMIC_ACQUIRE, "wavefront");
}
DEV float silu_f(float g) {
  return g * __builtin_amdgcn_rcpf(1.0f + __builtin_amdgcn_exp2f(g * -1.44269504088896340736f));
}
DEV void tr8x8(const u4* r, u4* o) {
#pragma unroll
  for (int c2 = 0; c2 < 4; ++c2) {
#pragma unroll
    for (int k2 = 0; k2 < 4; ++k2) {
      unsigned a = r[2 * k2][c2], b = r[2 * k2 + 1][c2];
      o[2 * c2][k2] = __builtin_amdgcn_perm(b, a, 0x05040100u);
      o[2 * c2 + 1][k2] = __builtin_amdgcn_perm(b, a, 0x07060302u);
    }
  }
}
typedef __attribute__((address_space(3))) s4v* lds_s4p;
DEV bf16x8 tr_frag(const char* p0, const char* p1) {
  s4v a = __builtin_amdgcn_ds_read_tr16_b64_v4i16((lds_s4p)p0);
  s4v b = __builtin_amdgcn_ds_read_tr16_b64_v4i16((lds_s4p)p1);
  return __builtin_shufflevector(a, b, 0, 1, 2, 3, 4, 5, 6, 7);
}

typedef __amdgpu_buffer_rsrc_t rsrc_t;
DEV rsrc_t make_rsrc(void* p, unsigned bytes) { return __builtin_amdgcn_make_buffer_rsrc(p, 0, bytes, 0x00020000); }
constexpr int ST_AUX = 16;
template <int AUX>
DEV void bst16(rsrc_t r, unsigned off, u4 v) { __builtin_amdgcn_raw_buffer_store_b128(v, r, off, 0, AUX); }
template <int AUX>
DEV void bst8(rsrc_t r, unsigned off, u2 v) { __builtin_amdgcn_raw_buffer_store_b64(v, r, off, 0, AUX); }

constexpr int LDS_BYTES = 80 * 1024;
template <int N>
struct IC {
  static constexpr int value = N;
};

typedef _Float16 h8 __attribute__((ext_vector_type(8)));
typedef _Float16 h2 __attribute__((ext_vector_type(2)));
DEV unsigned pk_f16(float a, float b) {
  h2 v;
  v.x = (_Float16)a;
  v.y = (_Float16)b;
  return __builtin_bit_cast(unsigned, v);
}
DEV f4 mfma_h(u4 a, u4 b) {
  return __builtin_amdgcn_mfma_f32_16x16x32_f16(__builtin_bit_cast(h8, a), __builtin_bit_cast(h8, b),
                                                f4{0.f, 0.f, 0.f, 0.f}, 0, 0, 0);
}
template <int SGN>
DEV void dft_mats(const float* ang, float sc, u4& fre, u4& fim) {
#pragma unroll
  for (int e = 0; e < 4; ++e) {
    const float c = __builtin_amdgcn_cosf(ang[e]) * sc, s = __builtin_amdgcn_sinf(ang[e]) * sc;
    fre[e] = pk_f16(c, SGN < 0 ? s : -s);
    fim[e] = pk_f16(SGN < 0 ? -s : s, c);
  }
}
template <int SGN>
DEV u4 stage_tw(u4 d, u4 fre, u4 fim, const f2* tw) {
  const f4 re = mfma_h(d, fre), im = mfma_h(d, fim);
  u4 o;
#pragma unroll
  for (int r = 0; r < 4; ++r) {
    const f2 v = SGN < 0 ? cmul(f2{re[r], im[r]}, tw[r]) : cmulc(f2{re[r], im[r]}, tw[r]);
    o[r] = pk_f16(v.x, v.y);
  }
  return o;
}
DEV int wmax_exp(int m, int lane) {
  int e = max64(m) >> 7;
  e = e < 16 ? 16 : e;
  e = e > 240 ? 240 : e;
  return e;
}
template <int SGN>
struct RowConsts {
  u4 f16re, f16im, f8re, f8im;
  f2 t128[4], t640[4][4];
  DEV void init(int lane) {
    const int c = lane & 15, q = lane >> 4;
    float ang[4];
#pragma unroll
    for (int e = 0; e < 4; ++e) ang[e] = (float)(((4 * q + e) * c) & 15) * (1.0f / 16.0f);
    dft_mats<SGN>(ang, SGN > 0 ? 0.25f : 1.0f, f16re, f16im);
#pragma unroll
    for (int e = 0; e < 4; ++e) ang[e] = (float)(((4 * (q & 1) + e) * (c & 7)) & 7) * (1.0f / 8.0f);
    dft_mats<SGN>(ang, (q >> 1) == (c >> 3) ? 1.0f : 0.0f, f8re, f8im);
#pragma unroll
    for (int r = 0; r < 4; ++r) t128[r] = twid((float)((4 * (q & 1) + r) * c) * (1.0f / 128.0f));
#pragma unroll
    for (int r = 0; r < 4; ++r)
#pragma unroll
      for (int j = 1; j < 5; ++j) t640[r][j - 1] = twid((float)(j * (16 * (c & 7) + 4 * q + r)) * (1.0f / 640.0f));
  }
  DEV void run(const u4* d, f2 (*o)[5]) const {
    f4 re[5], im[5];
#pragma unroll
    for (int j = 0; j < 5; ++j) {
      const u4 m = stage_tw<SGN>(d[j], f16re, f16im, t128);
      re[j] = mfma_h(m, f8re);
      im[j] = mfma_h(m, f8im);
    }
#pragma unroll
    for (int r = 0; r < 4; ++r) {
      f2 y[5];
      y[0] = f2{re[0][r], im[0][r]};
#pragma unroll
      for (int j = 1; j < 5; ++j)
        y[j] = SGN < 0 ? cmul(f2{re[j][r], im[j][r]}, t640[r][j - 1]) : cmulc(f2{re[j][r], im[j][r]}, t640[r][j - 1]);
      dft5<SGN>(y[0], y[1], y[2], y[3], y[4], o[r][0], o[r][1], o[r][2], o[r][3], o[r][4]);
    }
  }
};
#define TOUCH_CODE(t, idx, sym, len)                                                                        \
  do {                                                                                                      \
    unsigned _lo, _hi;                                                                                      \
    asm volatile("s_getpc_b64 s[20:21]\n\ts_add_u32 s20, s20, " sym "@rel32@lo+4\n\ts_addc_u32 s21, s21, " sym \
                 "@rel32@hi+12\n\ts_mov_b32 %0, s20\n\ts_mov_b32 %1, s21"                                   \
                 : "=s"(_lo), "=s"(_hi)::"scc", "s20", "s21");                                              \
    const char* _p = (const char*)(((unsigned long long)_hi << 32) | _lo);                                  \
    const unsigned _o = (unsigned)(idx) * 64u;                                                              \
    t = *(const unsigned*)(_p + (_o < (len) ? _o : 0u));                                                    \
  } while (0)
#define K0_SYM "_Z9k0_kernelPKtS0_S0_S0_S0_S0_Pc", 11264u
#define K1_SYM "_Z9k1_kernelPKtS0_Pc", 11008u
#define K2_SYM "_Z9k2_kernelPcPKtPt", 8960u
#define TOUCH_CODE2(t, idx, s) TOUCH_CODE(t, idx, s)
DEV int vtok_row(int v) {
  const int blk = v >> 4, i = v & 15;
  const int w = blk >> 5, q = (blk >> 3) & 3, a = (blk >> 1) & 3, z = blk & 1;
  return 2 * w + z + 8 * (4 * a + (i >> 2)) + 128 * (4 * q + (i & 3));
}

constexpr int NT0 = 512;
constexpr int K0_ROW_BLOCKS = 128;
constexpr int K0_WT_BLOCKS = 64;
constexpr int K0_W2_BLOCKS = (F_ * D_ / 64) / NT0;
constexpr int K0_TOUCH_BLOCKS = 34;
#define TOUCH_CHUNK(c) (((c) < 122880 ? (const u4*)w1 : (const u4*)w3)[(c) < 122880 ? (c) : ((c) < 245760 ? (c) - 122880 : 0)])
constexpr int ZL_RS = 656;
constexpr int ZS_RS = 720;
constexpr int K0_LDS = 8 * 8448;

__global__ __launch_bounds__(NT0) void k0_kernel(const unsigned short* __restrict__ x,
                                                 const unsigned short* __restrict__ wr,
                                                 const unsigned short* __restrict__ wi,
                                                 const unsigned short* __restrict__ w1,
                                                 const unsigned short* __restrict__ w3,
                                                 const unsigned short* __restrict__ w2, char* __restrict__ ws) {
  extern __shared__ __attribute__((aligned(16))) char smem[];
  const int tid = TIDX;
  int b = BIDX;
  const rsrc_t wsr = make_rsrc(ws, (unsigned)WS_USED);
  unsigned tc0 = 0;
  if (b >= 240 && tid < 256) TOUCH_CODE2(tc0, tid, K0_SYM);
  if (b < K0_ROW_BLOCKS) {
    unsigned* zl = (unsigned*)smem;
    f2* zs = (f2*)smem;
    char* xl = smem + 8 * ZL_RS * 4;
    const int blk = (b & 7) * 16 + (b >> 3);
    const int wv = tid >> 6, lane = tid & 63;
    RowConsts<-1> rc;
    {
      const int half = lane >> 5, c = lane & 31;
      const int vi = 2 * wv + half;
      const int row = vtok_row(blk * 16 + vi);
      const u2* xr = (const u2*)(x + (size_t)row * D_);
      u2 v[5];
#pragma unroll
      for (int i = 0; i < 5; ++i) v[i] = xr[c + 32 * i];
      SCHED0;
      if (wv < 4) rc.init(lane);
      else if (b == 0 && wv == 4) *(unsigned*)(ws + OFF_CNT + lane * 256) = 0u;
      SCHED0;
      float f[20];
#pragma unroll
      for (int i = 0; i < 5; ++i) {
        f[4 * i + 0] = bflo(v[i].x); f[4 * i + 1] = bfhi(v[i].x);
        f[4 * i + 2] = bflo(v[i].y); f[4 * i + 3] = bfhi(v[i].y);
      }
      float sum = 0.f;
#pragma unroll
      for (int i = 0; i < 20; ++i) sum += f[i];
      sum = sum32(sum);
      const float mean = sum * (1.0f / D_);
      float sq = 0.f;
#pragma unroll
      for (int i = 0; i < 20; ++i) { float d = f[i] - mean; sq += d * d; }
      sq = sum32(sq);
      const float mb = rbf(mean);
      const float vb = rbf(sq * (1.0f / D_));
      const float den = rbf(__builtin_sqrtf(rbf(vb + 1e-5f)));
      const float rcp = 1.0f / den;
#pragma unroll
      for (int i = 0; i < 5; ++i) {
        const unsigned d01 = pk_bf16(f[4 * i] - mb, f[4 * i + 1] - mb);
        const unsigned d23 = pk_bf16(f[4 * i + 2] - mb, f[4 * i + 3] - mb);
        u2 o;
        o.x = pk_bf16(bflo(d01) * rcp, bfhi(d01) * rcp);
        o.y = pk_bf16(bflo(d23) * rcp, bfhi(d23) * rcp);
        const int n = 4 * (c + 32 * i);
        const int ks = n >> 5, qq = (n >> 3) & 3, hf = (n >> 2) & 1;
        *(u2*)(xl + (ks * 64 + qq * 16 + vi) * 16 + hf * 8) = o;
        const unsigned h01 = pk_f16(bflo(o.x), bfhi(o.x)), h23 = pk_f16(bflo(o.y), bfhi(o.y));
        const auto sw = __builtin_amdgcn_permlane32_swap(h01, h23, false, false);
        const unsigned za = sw[0], zb = sw[1];
        *(u2*)(zl + wv * ZL_RS + n + 2 * half) =
            u2{__builtin_amdgcn_perm(zb, za, 0x05040100u), __builtin_amdgcn_perm(zb, za, 0x07060302u)};
      }
    }
    block_sync();
    u4 d[5];
    if (wv < 4) {
      const int pr = 2 * wv + ((lane >> 3) & 1), q = lane >> 4;
      const unsigned* zr = zl + pr * ZL_RS + 160 * q + 5 * (lane & 7);
#pragma unroll
      for (int j = 0; j < 5; ++j)
#pragma unroll
        for (int e = 0; e < 4; ++e) d[j][e] = zr[j + 40 * e];
    } else {
#pragma unroll
      for (int i = 0; i < 5; ++i) d[i] = *(const u4*)(xl + ((wv - 4) * 5 + i) * 1024 + lane * 16);
    }
    block_sync();
    if (wv < 4) {
      const int pr = 2 * wv + ((lane >> 3) & 1), q = lane >> 4;
      f2 o[4][5];
      rc.run(d, o);
      f2* zo = zs + pr * ZS_RS + 18 * (lane & 7) + 4 * q;
#pragma unroll
      for (int k0 = 0; k0 < 5; ++k0) {
        *(f4*)(zo + 144 * k0) = f4{o[0][k0].x, o[0][k0].y, o[1][k0].x, o[1][k0].y};
        *(f4*)(zo + 144 * k0 + 2) = f4{o[2][k0].x, o[2][k0].y, o[3][k0].x, o[3][k0].y};
      }
    } else {
#pragma unroll
      for (int i = 0; i < 5; ++i) {
        const int ks = (wv - 4) * 5 + i;
        bst16<ST_AUX>(wsr, (unsigned)OFF_XNP + ((((blk >> 3) * 20 + ks) * 8 + (blk & 7)) * 64 + lane) * 16, d[i]);
      }
    }
    block_sync();
    {
      const int w = blk >> 5, q = (blk >> 3) & 3, a = (blk >> 1) & 3, z = blk & 1;
      const unsigned xo = (unsigned)OFF_XT + (((2 * w + z) * 4 + q) * 16 + 4 * a) * 16;
#pragma unroll
      for (int it = 0; it < 3; ++it) {
        const int item = tid + it * NT0;
        if (item < DF_ * 4) {
          const int k = item >> 2, n2l = item & 3;
          const int km = k == 0 ? 0 : 640 - k;
          const int pk = k + 2 * (k >> 4), pm = km + 2 * (km >> 4);
          u4 o;
#pragma unroll
          for (int h = 0; h < 2; ++h) {
            const f2 za = zs[(2 * n2l + h) * ZS_RS + pk], zb = zs[(2 * n2l + h) * ZS_RS + pm];
            o[2 * h] = pk_f16(za.x + zb.x, za.y - zb.y);
            o[2 * h + 1] = pk_f16(za.y + zb.y, zb.x - za.x);
          }
          bst16<ST_AUX>(wsr, xo + k * 8192 + n2l * 16, o);
        }
      }
    }
    return;
  }
  b -= K0_ROW_BLOCKS;
  __builtin_amdgcn_s_sleep(18);
  if (b < K0_WT_BLOCKS) {
    constexpr int CH = 32 * DF_ * 2 / 16;
    const u4* sr = (const u4*)(wr + (size_t)b * 32 * DF_);
    const u4* si = (const u4*)(wi + (size_t)b * 32 * DF_);
    u4* lr = (u4*)smem;
    u4* li = lr + CH;
    u4 vr[3], vi[3];
    int* smax = (int*)(smem + 2 * CH * 16);
#pragma unroll
    for (int i = 0; i < 3; ++i) {
      const int c = tid + i * NT0;
      vr[i] = u4{0u, 0u, 0u, 0u};
      vi[i] = u4{0u, 0u, 0u, 0u};
      if (c < CH) { vr[i] = sr[c]; vi[i] = si[c]; }
    }
    u4 tv[3];
#pragma unroll
    for (int i = 0; i < 3; ++i) {
      const int c = (b * 3 + i) * NT0 + tid;
      tv[i] = TOUCH_CHUNK(c);
    }
    int mx = 0;
#pragma unroll
    for (int i = 0; i < 3; ++i) {
      const int c = tid + i * NT0;
      if (c < CH) { lr[c] = vr[i]; li[c] = vi[i]; }
#pragma unroll
      for (int e = 0; e < 4; ++e) {
        const int v0 = vr[i][e] & 0x7fff, v1 = (vr[i][e] >> 16) & 0x7fff;
        const int v2 = vi[i][e] & 0x7fff, v3 = (vi[i][e] >> 16) & 0x7fff;
        const int m0 = v0 > v1 ? v0 : v1, m1 = v2 > v3 ? v2 : v3;
        mx = mx > m0 ? mx : m0;
        mx = mx > m1 ? mx : m1;
      }
    }
    mx = max64(mx);
    if ((tid & 63) == 0) smax[tid >> 6] = mx;
    block_sync();
    if (tid == 0) {
#pragma unroll
      for (int i = 1; i < 8; ++i) mx = smax[i] > mx ? smax[i] : mx;
      *(int*)(ws + OFF_WMAX + b * 4) = mx;
    }
    const unsigned short* hr = (const unsigned short*)lr;
    const unsigned short* hi = (const unsigned short*)li;
#pragma unroll 3
    for (int i = 0; i < 6; ++i) {
      const int it = tid + i * NT0;
      if (it < DF_ * 8) {
        const int col = it >> 3, kq = it & 7;
        unsigned o[4];
#pragma unroll
        for (int e = 0; e < 4; ++e) {
          const int idx = (4 * kq + e) * DF_ + col;
          o[e] = (unsigned)hr[idx] | ((unsigned)hi[idx] << 16);
        }
        bst16<ST_AUX>(wsr, (unsigned)OFF_WT + (col * S_ + b * 32 + 4 * kq) * 4, u4{o[0], o[1], o[2], o[3]});
      }
    }
#pragma unroll
    for (int i = 0; i < 3; ++i) asm volatile("" ::"v"(tv[i]));
    return;
  }
  b -= K0_WT_BLOCKS;
  if (b < K0_W2_BLOCKS) {
    const int lane = tid & 63, task = b * 8 + (tid >> 6);
    const int ks = task / 5, dgrp = task - ks * 5;
    const int q = lane >> 4, dg = lane & 15;
    const int kq = ks * 4 + q;
    const int ci = kq % 12, cc = ci % 3, fb0 = (kq / 12) * 96 + (ci / 3) * 24;
    u4 rr[8], o[8];
#pragma unroll
    for (int j = 0; j < 8; ++j) {
      const int f = fb0 + (cc < 2 ? 8 * (j >> 2) + 4 * cc + (j & 3) : 16 + j);
      rr[j] = *(const u4*)(w2 + (size_t)f * D_ + dgrp * 128 + dg * 8);
    }
    tr8x8(rr, o);
    char* st = smem + (tid >> 6) * 8448;
#pragma unroll
    for (int c = 0; c < 8; ++c) *(u4*)(st + (dg >> 1) * 1056 + (q * 16 + (dg & 1) * 8 + c) * 16) = o[c];
    wave_sync();
#pragma unroll
    for (int fr = 0; fr < 8; ++fr) {
      const u4 v = *(const u4*)(st + fr * 1056 + lane * 16);
      const int D = dgrp * 8 + fr, nb = D / 5, dt = D - nb * 5;
      bst16<ST_AUX>(wsr, (unsigned)OFF_W2P + (((nb * 48 + ks) * 5 + dt) * 64 + lane) * 16, v);
    }
    return;
  }
  b -= K0_W2_BLOCKS;
  {
    unsigned tc;
    if (tid < 256) TOUCH_CODE2(tc, tid, K1_SYM);
    else TOUCH_CODE2(tc, tid - 256, K2_SYM);
    u4 v[9];
#pragma unroll
    for (int i = 0; i < 9; ++i) {
      const int c = K0_WT_BLOCKS * 3 * NT0 + (b * 9 + i) * NT0 + tid;
      v[i] = TOUCH_CHUNK(c);
    }
#pragma unroll
    for (int i = 0; i < 9; ++i) asm volatile("" ::"v"(v[i]));
    asm volatile("" ::"v"(tc), "v"(tc0));
  }
}

constexpr int G1_NS = 4;
constexpr int G1_STAGE = 20 * 1024;
constexpr int COL_LDS = 8 * 288 * 8;
constexpr int K1_LDS = G1_NS * G1_STAGE;
constexpr int K1_AUX = 16;

DEV void colfft_block(int col, char* __restrict__ ws, f2* E) {
  const int tid = TIDX & 255, wv = tid >> 6, lane = tid & 63;
  const int c = lane & 15, q = lane >> 4;
  u4 d[2];
#pragma unroll
  for (int z = 0; z < 2; ++z) d[z] = *(const u4*)(ws + OFF_XT + (size_t)col * 8192 + (2 * wv + z) * 1024 + lane * 16);
  const int wm = *(const int*)(ws + OFF_WMAX + lane * 4);
  unsigned wq[8];
  {
    const unsigned* wt = (const unsigned*)(ws + OFF_WT) + (size_t)col * S_ + tid;
#pragma unroll
    for (int k0 = 0; k0 < 8; ++k0) wq[k0] = wt[256 * k0];
  }
  SCHED0;
  u4 ffre, ffim, fire, fiim;
  f2 t256[4], t2k[7];
  {
    float ang[4];
#pragma unroll
    for (int e = 0; e < 4; ++e) ang[e] = (float)(((4 * q + e) * c) & 15) * (1.0f / 16.0f);
    dft_mats<-1>(ang, 1.0f, ffre, ffim);
    dft_mats<1>(ang, 0.25f, fire, fiim);
#pragma unroll
    for (int r = 0; r < 4; ++r) t256[r] = twid((float)((4 * q + r) * c) * (1.0f / 256.0f));
#pragma unroll
    for (int n0 = 1; n0 < 8; ++n0) t2k[n0 - 1] = twid((float)(n0 * tid) * (1.0f / 2048.0f));
  }
  SCHED0;
  const int we = wmax_exp(wm, lane);
#pragma unroll
  for (int z = 0; z < 2; ++z) {
    const u4 m = stage_tw<-1>(d[z], ffre, ffim, t256);
    const f4 re = mfma_h(m, ffre), im = mfma_h(m, ffim);
    f2* e = E + (2 * wv + z) * 288 + 18 * c + 4 * q;
    *(f4*)(e) = f4{re[0], im[0], re[1], im[1]};
    *(f4*)(e + 2) = f4{re[2], im[2], re[3], im[3]};
  }
  block_sync();
  {
    f2* e = E + 18 * (tid >> 4) + (tid & 15);
    f2 z[8];
    z[0] = e[0];
#pragma unroll
    for (int n0 = 1; n0 < 8; ++n0) z[n0] = cmul(e[288 * n0], t2k[n0 - 1]);
    dft8<-1>(z);
    const float wsc = U2F((unsigned)(126 - 9 + 127 - we) << 23);
#pragma unroll
    for (int k0 = 0; k0 < 8; ++k0) z[k0] = cmul(z[k0], f2{bflo(wq[k0]) * wsc, bfhi(wq[k0]) * wsc});
    dft8<1>(z);
    *(unsigned*)(e) = pk_f16(z[0].x, z[0].y);
#pragma unroll
    for (int n0 = 1; n0 < 8; ++n0) {
      const f2 v = cmulc(z[n0], t2k[n0 - 1]);
      *(unsigned*)(e + 288 * n0) = pk_f16(v.x, v.y);
    }
  }
  block_sync();
  f4 yre[2], yim[2];
#pragma unroll
  for (int z = 0; z < 2; ++z) {
    const f2* e = E + (2 * wv + z) * 288 + 72 * q + c;
    u4 g;
#pragma unroll
    for (int i = 0; i < 4; ++i) g[i] = *(const unsigned*)(e + 18 * i);
    const u4 m = stage_tw<1>(g, fire, fiim, t256);
    yre[z] = mfma_h(m, fire);
    yim[z] = mfma_h(m, fiim);
  }
  const rsrc_t wsr = make_rsrc(ws, (unsigned)WS_USED);
  const unsigned vo = (unsigned)OFF_VT + tid * 16;
  if (col == 0 || col == 320) {
    u4 o;
#pragma unroll
    for (int r = 0; r < 4; ++r) o[r] = pk_f16(yre[0][r], yre[1][r]);
    bst16<K1_AUX>(wsr, vo + col * 4096, o);
  } else {
    u4 o, om;
#pragma unroll
    for (int r = 0; r < 4; ++r) {
      o[r] = pk_f16(yre[0][r] - yim[1][r], yim[0][r] + yre[1][r]);
      om[r] = pk_f16(yre[0][r] + yim[1][r], yre[1][r] - yim[0][r]);
    }
    bst16<K1_AUX>(wsr, vo + col * 4096, o);
    bst16<K1_AUX>(wsr, vo + (640 - col) * 4096, om);
  }
}

__global__ __launch_bounds__(512, 4) void k1_kernel(const unsigned short* __restrict__ w1,
                                                    const unsigned short* __restrict__ w3, char* __restrict__ ws) {
  extern __shared__ __attribute__((aligned(16))) char smem[];
  const int b = BIDX;
  const int tid = TIDX, lane = tid & 63;
  const int wv = __builtin_amdgcn_readfirstlane(tid >> 6);
  if (b >= 256) {
    unsigned tc = 0;
    if (tid < 256) TOUCH_CODE2(tc, tid, K1_SYM);
    int col = 2 * (b - 256) + (wv >> 2);
    col = col > 320 ? 320 : col;
    colfft_block(col, ws, (f2*)(smem + (wv >> 2) * COL_LDS));
    asm volatile("" ::"v"(tc));
    return;
  }
  const int xc = b & 7, idx = b >> 3;
  const int mb = (xc & 1) * 8 + (idx & 7), nb = (xc >> 1) * 4 + (idx >> 3);
  const unsigned l16 = lane * 16;
  unsigned ldsw = __builtin_amdgcn_readfirstlane(
      (unsigned)(uintptr_t)(__attribute__((address_space(3))) char*)smem + wv * 1024);
  asm volatile("" : "+s"(ldsw));
  unsigned voff[3];
  const char* sbase[3];
  int sstride[3];
#pragma unroll
  for (int c = 0; c < 3; ++c) {
    const int i = wv + 8 * c;
    const int mat = i >= 6 ? 1 : 0, i6 = i - 6 * mat;
    const int P = i6 * 64 + lane, r = P / 12, ch = P - r * 12;
    const unsigned wo = r * 3072 + ((ch ^ (2 * ((r >> 3) & 1) + mat)) * 16);
    const bool isw = i < 12;
    voff[c] = isw ? wo : l16;
    sbase[c] = isw ? (const char*)(mat ? w3 : w1) + nb * 192 : ws + OFF_XNP + (size_t)mb * 20 * 8192 + (i - 12) * 1024;
    sstride[c] = isw ? 32 * 3072 : 8192;
  }
  auto stage1 = [&](int np, int ks, int sl, int c) {
    if (c < np) GLDSB(voff[c], sbase[c] + ks * sstride[c], ldsw, sl * G1_STAGE + c * 8192);
  };
  const int wm = wv >> 2, wn = wv & 3;
  f4 acc[3][4];
#pragma unroll
  for (int i = 0; i < 3; ++i)
#pragma unroll
    for (int t = 0; t < 4; ++t) acc[i][t] = f4{0.f, 0.f, 0.f, 0.f};
  bf16x8 fa[2][3], fb[2][4];
  unsigned la[3], lb;
  {
    const int lg = lane >> 4, p = lane & 3, m = p >> 1;
    const int arow = m * 6144 + (8 * lg + ((lane & 15) >> 2)) * 192 + (p & 1) * 8;
#pragma unroll
    for (int i = 0; i < 3; ++i) la[i] = arow + (((wn * 3 + i) ^ (2 * (lg & 1) + m)) * 16);
    lb = 12288 + wm * 4096 + l16;
    asm volatile("" : "+v"(lb));
  }
  auto ldA = [&](int sl, int i) {
    const char* p = smem + la[i] + sl * G1_STAGE;
    fa[sl & 1][i] = tr_frag(p, p + 4 * 192);
  };
  auto ldB = [&](int sl, int t) { fb[sl & 1][t] = *(const bf16x8*)(smem + lb + sl * G1_STAGE + t * 1024); };
  auto step = [&](int np, int ks, int sl, bool more, bool refill) {
    SCHED0;
    const int p = sl & 1, ns = (sl + 1) % G1_NS;
#pragma unroll
    for (int i = 0; i < 3; ++i) {
#pragma unroll
      for (int hf = 0; hf < 2; ++hf) {
        const int slot = i * 2 + hf;
#pragma unroll
        for (int t = 2 * hf; t < 2 * hf + 2; ++t)
          acc[i][t] = __builtin_amdgcn_mfma_f32_16x16x32_bf16(fa[p][i], fb[p][t], acc[i][t], 0, 0, 0);
        if (more) {
          if (slot < 2) { ldB(ns, 2 * slot); ldB(ns, 2 * slot + 1); }
          else if (slot < 5) ldA(ns, slot - 2);
        }
        if (refill && (slot & 1)) stage1(np, ks + G1_NS, sl, slot >> 1);
        SCHED0;
      }
    }
  };
  auto mainloop = [&](auto npc) {
    constexpr int NP = decltype(npc)::value;
    constexpr int np = NP;
#pragma unroll
    for (int ks = 0; ks < G1_NS; ++ks)
#pragma unroll
      for (int c = 0; c < 3; ++c) stage1(np, ks, ks, c);
    WAITVA_I(3 * NP);
    BARRIER;
#pragma unroll
    for (int t = 0; t < 4; ++t) ldB(0, t);
#pragma unroll
    for (int i = 0; i < 3; ++i) ldA(0, i);
#pragma unroll 1
    for (int ko = 0; ko < 16; ko += 4) {
#pragma unroll
      for (int sl = 0; sl < 4; ++sl) {
        WAITVA_I(2 * NP);
        WAITL0;
        BARRIER;
        step(np, ko + sl, sl, true, true);
      }
    }
    WAITVA_I(2 * NP); WAITL0; BARRIER;
    step(np, 16, 0, true, false);
    WAITVA_I(NP); WAITL0; BARRIER;
    step(np, 17, 1, true, false);
  };
  if (wv < 4) mainloop(IC<3>{});
  else mainloop(IC<2>{});
  WAITVA(0); WAITL0; BARRIER;
  step(0, 18, 2, true, false);
  step(0, 19, 3, false, false);
  const rsrc_t wsr = make_rsrc(ws, (unsigned)WS_USED);
  const int q = (lane >> 4) & 1;
  const int c0 = wn * 3 + q, c2 = wn * 3 + 2;
  unsigned h2[2][2];
  const unsigned hbase = (unsigned)OFF_HP + (((mb * 2 + wm) * 48 + nb * 3) * 4 + (lane >> 5)) * 1024 + (lane & 15) * 16;
#pragma unroll
  for (int tp = 0; tp < 2; ++tp) {
    unsigned hw[3][2];
#pragma unroll
    for (int i = 0; i < 3; ++i) {
      float gt[4], ut[4];
#pragma unroll
      for (int r = 0; r < 4; ++r) {
        const auto sw = __builtin_amdgcn_permlane32_swap(F2U(acc[i][2 * tp][r]), F2U(acc[i][2 * tp + 1][r]), false, false);
        gt[r] = U2F(sw[0]);
        ut[r] = U2F(sw[1]);
      }
#pragma unroll
      for (int r2 = 0; r2 < 2; ++r2) {
        hw[i][r2] = pk_bf16(silu_f(gt[2 * r2]) * ut[2 * r2], silu_f(gt[2 * r2 + 1]) * ut[2 * r2 + 1]);
      }
    }
    bst16<K1_AUX>(wsr, hbase + tp * 2048 + (c0 >> 2) * 4096 + (c0 & 3) * 256, u4{hw[0][0], hw[0][1], hw[1][0], hw[1][1]});
    h2[tp][0] = hw[2][0];
    h2[tp][1] = hw[2][1];
  }
  {
    const auto s0 = __builtin_amdgcn_permlane16_swap(h2[0][0], h2[1][0], false, false);
    const auto s1 = __builtin_amdgcn_permlane16_swap(h2[0][1], h2[1][1], false, false);
    bst16<K1_AUX>(wsr, hbase + q * 2048 + (c2 >> 2) * 4096 + (c2 & 3) * 256, u4{s0[0], s1[0], s0[1], s1[1]});
  }
}

#define S_ARRIVALS 8u
constexpr int NT2 = 384;
constexpr int G2_NS = 4;
constexpr int G2_STAGE = 18 * 1024;
typedef __attribute__((address_space(1))) unsigned gu32;

__global__ __launch_bounds__(NT2, 3) void k2_kernel(char* __restrict__ ws, const unsigned short* __restrict__ x,
                                                    unsigned short* __restrict__ out) {
  extern __shared__ __attribute__((aligned(16))) char smem[];
  const int tid = TIDX, lane = tid & 63;
  const int wv = __builtin_amdgcn_readfirstlane(tid >> 6);
  int b = BIDX;
  const rsrc_t wsr = make_rsrc(ws, (unsigned)WS_USED);
  if (b >= 256) {
    b -= 256;
    __builtin_amdgcn_s_setprio(2);
    if (wv >= 2) {
      unsigned tc;
      TOUCH_CODE2(tc, tid - 128, K2_SYM);
      asm volatile("" ::"v"(tc));
      return;
    }
    const int pos = (b & 7) * 32 + (b >> 3);
    unsigned* vl = (unsigned*)smem;
    RowConsts<1> rc;
#pragma unroll
    for (int i = 0; i < 5; ++i) {
      const int k = (wv * 5 + i) * 64;
      GLDS(ws + OFF_VT + (size_t)(k + lane) * 4096 + pos * 16, smem + k * 16, 0);
    }
    const int we = wmax_exp(*(const int*)(ws + OFF_WMAX + lane * 4), lane);
    rc.init(lane);
    WAITV(0);
    BARRIER;
    const int sub = (lane >> 3) & 1, q = lane >> 4;
    const int r = 2 * wv + sub;
    const unsigned* vr = vl + 4 * (160 * q + 5 * (lane & 7)) + r;
    u4 d[5];
#pragma unroll
    for (int j = 0; j < 5; ++j)
#pragma unroll
      for (int e = 0; e < 4; ++e) d[j][e] = vr[4 * (j + 40 * e)];
    f2 o[4][5];
    rc.run(d, o);
    const float fs = U2F((unsigned)(we + 9 + 1) << 23) * (32.0f / (2048.0f * 640.0f));
    const int rowa = 2 * (pos >> 6) + 8 * (4 * ((pos >> 4) & 3) + r) + 128 * (pos & 15);
    const unsigned so = (unsigned)OFF_S + (rowa * D_ + 16 * (lane & 7) + 4 * q) * 2;
#pragma unroll
    for (int n0 = 0; n0 < 5; ++n0) {
      bst8<16>(wsr, so + n0 * 256,
               u2{pk_bf16(o[0][n0].x * fs, o[1][n0].x * fs), pk_bf16(o[2][n0].x * fs, o[3][n0].x * fs)});
      bst8<16>(wsr, so + n0 * 256 + D_ * 2,
               u2{pk_bf16(o[0][n0].y * fs, o[1][n0].y * fs), pk_bf16(o[2][n0].y * fs, o[3][n0].y * fs)});
    }
    WAITVA(0);
    BARRIER;
    if (tid == 0) {
      const int mbc = ((pos >> 6) * 4 + ((pos & 15) >> 2)) * 2 + ((pos >> 5) & 1);
      gu32* c = (gu32*)(ws + OFF_CNT + mbc * 256);
      __hip_atomic_fetch_add(c, 1u, __ATOMIC_RELAXED, __HIP_MEMORY_SCOPE_AGENT);
    }
    return;
  }
  const int xc = b & 7, idx = b >> 3;
  const int mb = (xc >> 1) * 8 + (idx & 7), nb = (xc & 1) * 4 + (idx >> 3);
  const unsigned l16 = lane * 16;
  if (wv >= 4) {
    const int ld = wv - 4;
    const char* gh = ws + OFF_HP + ((size_t)mb * 48 + ld) * 4096 + l16;
    const char* gw = ws + OFF_W2P + ((size_t)nb * 48 + ld) * 5120 + l16;
    auto stage = [&](int st, int sl) {
      char* d = smem + sl * G2_STAGE + ld * 9216;
      const char* sh = gh + st * 8192;
      const char* sw = gw + st * 10240;
      GLDS(sh, d, 0); GLDS(sh, d, 1024); GLDS(sh, d, 2048); GLDS(sh, d, 3072);
      GLDS(sw, d + 4096, 0); GLDS(sw, d + 4096, 1024); GLDS(sw, d + 4096, 2048); GLDS(sw, d + 4096, 3072);
      GLDS(sw + 4096, d + 8192, 0);
    };
    stage(0, 0);
    stage(1, 1);
    stage(2, 2);
    stage(3, 3);
    WAITV(27);
    BARRIER;
#pragma unroll 1
    for (int so = 0; so < 20; so += 4) {
#pragma unroll
      for (int sl = 0; sl < 4; ++sl) {
        WAITV(18);
        BARRIER;
        stage(so + sl + G2_NS, sl);
      }
    }
    WAITV(18); BARRIER;
    WAITV(9); BARRIER;
    WAITV(0); BARRIER;
    return;
  }
  const int wm = wv >> 1, wk = wv & 1;
  const int tok = vtok_row(mb * 64 + wm * 32 + wk * 16 + (lane & 15));
  const unsigned eo = (tok * D_ + nb * 80 + (lane >> 4) * 4) * 2;
  u2 xv[5];
#pragma unroll
  for (int i = 0; i < 5; ++i) xv[i] = *(const u2*)((const char*)x + eo + i * 32);
  f4 acc[5][2];
#pragma unroll
  for (int i = 0; i < 5; ++i)
#pragma unroll
    for (int t = 0; t < 2; ++t) acc[i][t] = f4{0.f, 0.f, 0.f, 0.f};
  bf16x8 fa[2][5], fb[2][2];
  const char* lb0 = smem + wk * 9216 + wm * 2048 + wk * 1024 + l16;
  const char* lb1 = smem + wk * 9216 + wm * 2048 + (wk ^ 1) * 1024 + l16;
  const char* la = smem + wk * 9216 + 4096 + l16;
  auto ldfrag = [&](int sl) {
    const int o = sl * G2_STAGE, p = sl & 1;
    fb[p][0] = *(const bf16x8*)(lb0 + o);
    fb[p][1] = *(const bf16x8*)(lb1 + o);
#pragma unroll
    for (int i = 0; i < 5; ++i) fa[p][i] = *(const bf16x8*)(la + o + i * 1024);
  };
  auto step = [&](int sl, bool more) {
    if (more) {
      WAITL0;
      BARRIER;
      SCHED0;
      ldfrag((sl + 1) % G2_NS);
    }
    const int p = sl & 1;
#pragma unroll
    for (int i = 0; i < 5; ++i)
#pragma unroll
      for (int t = 0; t < 2; ++t)
        acc[i][t] = __builtin_amdgcn_mfma_f32_16x16x32_bf16(fa[p][i], fb[p][t], acc[i][t], 0, 0, 0);
    if (more) {
#pragma unroll
      for (int g = 0; g < 7; ++g) {
        __builtin_amdgcn_sched_group_barrier(0x8, 1, 0);
        __builtin_amdgcn_sched_group_barrier(0x100, 1, 0);
      }
      __builtin_amdgcn_sched_group_barrier(0x8, 3, 0);
    }
    SCHED0;
  };
  BARRIER;
  ldfrag(0);
#pragma unroll 1
  for (int so = 0; so < 20; so += 4) {
#pragma unroll
    for (int sl = 0; sl < 4; ++sl) step(sl, true);
  }
  if (wv == 0) {
    gu32* c = (gu32*)(ws + OFF_CNT + mb * 256);
    unsigned spins = 0;
    while ((unsigned)__builtin_amdgcn_readfirstlane(
               (int)__hip_atomic_load(c, __ATOMIC_RELAXED, __HIP_MEMORY_SCOPE_AGENT)) < S_ARRIVALS) {
      __builtin_amdgcn_s_sleep(2);
      if (++spins > (1u << 18)) break;
    }
  }
  step(0, true);
  u2 sv[5];
#pragma unroll
  for (int i = 0; i < 5; ++i)
    sv[i] = __builtin_amdgcn_raw_buffer_load_b64(wsr, (unsigned)OFF_S + eo + i * 32, 0, 16);
  step(1, true);
  step(2, true);
  step(3, false);
  block_sync();
  f4* red = (f4*)smem;
#pragma unroll
  for (int i = 0; i < 5; ++i) red[(wv * 5 + i) * 64 + lane] = acc[i][1];
  block_sync();
#pragma unroll
  for (int i = 0; i < 5; ++i) {
    const f4 o = red[((wv ^ 1) * 5 + i) * 64 + lane];
    const f4 m = acc[i][0];
    u2 res;
#pragma unroll
    for (int r2 = 0; r2 < 2; ++r2) {
      res[r2] = pk_bf16(__builtin_fmaf(bflo(sv[i][r2]), silu_f(o[2 * r2] + m[2 * r2]), bflo(xv[i][r2])),
                        __builtin_fmaf(bfhi(sv[i][r2]), silu_f(o[2 * r2 + 1] + m[2 * r2 + 1]), bfhi(xv[i][r2])));
    }
    *(u2*)((char*)out + eo + i * 32) = res;
  }
}

#if defined(__HIP_DEVICE_COMPILE__)
#pragma clang attribute pop
#endif

extern "C" void kernel_launch(void* const* d_in, const int* in_sizes, int n_in, void* d_out, int out_size, void* d_ws,
                              size_t ws_size, hipStream_t stream) {
  const unsigned short* x = (const unsigned short*)d_in[0];
  const unsigned short* wr = (const unsigned short*)d_in[1];
  const unsigned short* wi = (const unsigned short*)d_in[2];
  const unsigned short* w1 = (const unsigned short*)d_in[4];
  const unsigned short* w3 = (const unsigned short*)d_in[5];
  const unsigned short* w2 = (const unsigned short*)d_in[6];
  char* ws = (char*)d_ws;
  (void)hipFuncSetAttribute((const void*)k1_kernel, hipFuncAttributeMaxDynamicSharedMemorySize, K1_LDS);
  (void)hipFuncSetAttribute((const void*)k2_kernel, hipFuncAttributeMaxDynamicSharedMemorySize, LDS_BYTES);
  (void)hipFuncSetAttribute((const void*)k0_kernel, hipFuncAttributeMaxDynamicSharedMemorySize, K0_LDS);
  k0_kernel<<<K0_ROW_BLOCKS + K0_WT_BLOCKS + K0_W2_BLOCKS + K0_TOUCH_BLOCKS, NT0, K0_LDS, stream>>>(x, wr, wi, w1, w3, w2, ws);
  k1_kernel<<<256 + 161, 512, K1_LDS, stream>>>(w1, w3, ws);
  k2_kernel<<<512, NT2, LDS_BYTES, stream>>>(ws, x, (unsigned short*)d_out);
}
```
